```python
import torch

def _softmax(x, axis=-1):
    return torch.softmax(x, dim=axis)
def _gelu(x):
    return torch.nn.functional.gelu(x, approximate="tanh")
def _silu(x):
    return torch.nn.functional.silu(x)
def _sigmoid(x):
    return torch.sigmoid(x)
def _layernorm(x, axis=-1, eps=1e-5):
    m = x.mean(dim=axis, keepdim=True)
    v = x.var(dim=axis, keepdim=True, unbiased=False)
    return (x - m) / torch.sqrt(v + eps)
def _scatter_add(tgt, idx, src, axis=0):
    return tgt.clone().index_add_(axis, idx, src)
def _segment_sum(src, seg, n):
    out = torch.zeros((n,) + src.shape[1:], dtype=src.dtype)
    return out.index_add_(0, seg, src)
def _pad(x, pw):
    flat = tuple(v for pair in reversed(pw) for v in pair)
    return torch.nn.functional.pad(x, flat)

kernel_name = 'interleave_layers__linear_attn_block__dense_activation_gelu__prompt'

def setup_inputs():

    _w = (lambda a: (lambda t: t.to(torch.bfloat16) if t.is_floating_point() else t)(torch.as_tensor(a)))
    def _A_setup():
        B, S, D = 1, 2048, 640
        return {
            "x": _w((lambda *s: torch.randn(*s, dtype=torch.bfloat16) * 0.05)(B, S, D)),
            "wqkv": _w((lambda *s: torch.randn(*s, dtype=torch.bfloat16) * 0.05)(D, 3 * D)),
            "wo": _w((lambda *s: torch.randn(*s, dtype=torch.bfloat16) * 0.05)(D, D)),
        }
    _A_in = _A_setup()

    def _B_setup():
        B, S, D, F = 1, 2048, 640, 1728
        return {
            "x": _w((lambda *s: torch.randn(*s, dtype=torch.bfloat16) * 0.05)(B, S, D)),
            "w1": _w((lambda *s: torch.randn(*s, dtype=torch.bfloat16) * 0.8)(D, F) * (D ** -0.5)),
            "b1": _w((lambda *s: torch.randn(*s, dtype=torch.bfloat16) * 0.8)(F) * 0.01),
            "w2": _w((lambda *s: torch.randn(*s, dtype=torch.bfloat16) * 0.8)(F, D) * (F ** -0.5)),
            "b2": _w((lambda *s: torch.randn(*s, dtype=torch.bfloat16) * 0.8)(D) * 0.01),
        }
    _B_in = _B_setup()

    return {
        "A_x": _A_in["x"],
        "A_wqkv": _A_in["wqkv"],
        "A_wo": _A_in["wo"],
        "B_x": _B_in["x"],
        "B_w1": _B_in["w1"],
        "B_b1": _B_in["b1"],
        "B_w2": _B_in["w2"],
        "B_b2": _B_in["b2"],
    }

def reference(A_x, A_wqkv, A_wo, B_x, B_w1, B_b1, B_w2, B_b2):
    def _A_ref(x, wqkv, wo):
        B, S, D = x.shape
        H = 32; Dh = D // H
        qkv = (lambda x, *d: x.permute(*d))((x @ wqkv).reshape(B, S, 3, H, Dh), 2, 0, 3, 1, 4)
        q, k, v = qkv[0], qkv[1], qkv[2]
        phi = lambda t: torch.where(t > 0, t + 1, torch.exp(t)) + 1e-6
        pq, pk = phi(q), phi(k)
        kv = torch.einsum("bhsd,bhse->bhde", pk, v)
        z = torch.einsum("bhsd->bhd", pk)
        num = torch.einsum("bhqd,bhde->bhqe", pq, kv)
        den = torch.einsum("bhqd,bhd->bhq", pq, z)[..., None] + 1e-6
        o = num / den
        return (lambda x, *d: x.permute(*d))(o, 0, 2, 1, 3).reshape(B, S, D) @ wo

    def _B_ref(x, w1, b1, w2, b2):
        h = torch.einsum("bsd,df->bsf", x, w1) + b1
        h = _gelu(h)
        return torch.einsum("bsf,fd->bsd", h, w2) + b2

    x = A_x
    x = x + _A_ref(_layernorm(x, axis=-1), A_wqkv, A_wo)
    x = x + _B_ref(_layernorm(x, axis=-1), B_w1, B_b1, B_w2, B_b2)
    x = x + _A_ref(_layernorm(x, axis=-1), A_wqkv, A_wo)
    x = x + _B_ref(_layernorm(x, axis=-1), B_w1, B_b1, B_w2, B_b2)
    return x
```

```cpp
#include <hip/hip_runtime.h>
#include <hip/hip_bf16.h>
#include <stdint.h>

typedef unsigned short u16;
typedef __attribute__((ext_vector_type(4))) short s16x4;
typedef __attribute__((ext_vector_type(8))) short s16x8;
typedef __attribute__((ext_vector_type(4))) float f32x4;
typedef __attribute__((ext_vector_type(4))) unsigned u32x4;
typedef __attribute__((address_space(3))) s16x4* lds_s16x4_p;

constexpr int S_ = 2048, D_ = 640, H_ = 32, F_ = 1728;
constexpr int BM = 64, BK = 64, NTHR = 256;
constexpr int A_STRIDE = D_ * 2 + 16;
constexpr int KV_LD = 24;

__device__ __forceinline__ float bf2f(u16 v) { return __uint_as_float(((unsigned)v) << 16); }
__device__ __forceinline__ u16 f2bf(float f) {
  unsigned u = __float_as_uint(f);
  return (u16)((u + 0x7FFFu + ((u >> 16) & 1u)) >> 16);
}
__device__ __forceinline__ unsigned pack2(float a, float b) { return (unsigned)f2bf(a) | ((unsigned)f2bf(b) << 16); }
__device__ __forceinline__ float phi_f(float t) { return (t > 0.f ? t + 1.f : __expf(t)) + 1e-6f; }
__device__ __forceinline__ float gelu_f(float x) {
  float u = 0.7978845608028654f * (x + 0.044715f * x * x * x);
  float e = __expf(2.f * u);
  float th = 1.f - 2.f / (e + 1.f);
  return 0.5f * x * (1.f + th);
}
__device__ __forceinline__ int perm_row(int r) { return (r & ~12) | ((r & 4) << 1) | ((r & 8) >> 1); }

template <int BSTRIDE>
__device__ __forceinline__ s16x8 tr_frag(const char* img, int lane, int krow0, int col0) {
  const int g = lane >> 4, q = (lane & 15) >> 2, p = lane & 3;
  const int r = krow0 + q + 4 * (g & 1) + 16 * (g >> 1);
  const char* a0 = img + r * BSTRIDE + (col0 + 4 * p) * 2;
  s16x4 lo = __builtin_amdgcn_ds_read_tr16_b64_v4i16((lds_s16x4_p)(a0));
  s16x4 hi = __builtin_amdgcn_ds_read_tr16_b64_v4i16((lds_s16x4_p)(a0 + 8 * BSTRIDE));
  return __builtin_shufflevector(lo, hi, 0, 1, 2, 3, 4, 5, 6, 7);
}

template <int NCH, int NLD, int MODE>
__device__ __forceinline__ void b_load(u32x4 (&r)[NLD], const u16* __restrict__ W, int ldw, int k0, int n0, int nlim) {
#pragma unroll
  for (int i = 0; i < NLD; ++i) {
    const int id = threadIdx.x + i * NTHR;
    const int row = id / NCH, c = id % NCH;
    int gcol;
    if (MODE == 1) gcol = (c / 10) * D_ + n0 + (c % 10) * 8;
    else gcol = n0 + c * 8;
    u32x4 v = {0u, 0u, 0u, 0u};
    if (id < BK * NCH && gcol < nlim) v = *(const u32x4*)(W + (size_t)(k0 + row) * ldw + gcol);
    r[i] = v;
  }
}
template <int NCH, int NLD, int BSTRIDE>
__device__ __forceinline__ void b_store(const u32x4 (&r)[NLD], char* img) {
#pragma unroll
  for (int i = 0; i < NLD; ++i) {
    const int id = threadIdx.x + i * NTHR;
    const int row = id / NCH, c = id % NCH;
    if (id < BK * NCH) *(u32x4*)(img + perm_row(row) * BSTRIDE + c * 16) = r[i];
  }
}

template <int NCH, int NLD, int MODE, int BSTRIDE, int NTW>
__device__ __forceinline__ void mainloop_resA(f32x4 (&acc)[4][NTW], const char* As, char* Bs, const u16* __restrict__ W,
                                              int ldw, int n0, int nlim, int ntiles) {
  const int lane = threadIdx.x & 63, wid = threadIdx.x >> 6;
  constexpr int NT = D_ / BK;
  constexpr int BUF = BK * BSTRIDE;
  u32x4 r[NLD];
  b_load<NCH, NLD, MODE>(r, W, ldw, 0, n0, nlim);
  b_store<NCH, NLD, BSTRIDE>(r, Bs);
  b_load<NCH, NLD, MODE>(r, W, ldw, BK, n0, nlim);
  __syncthreads();
  for (int t = 0; t < NT; ++t) {
    const char* B = Bs + (t & 1) * BUF;
#pragma unroll
    for (int ks = 0; ks < 2; ++ks) {
      s16x8 a[4];
#pragma unroll
      for (int m = 0; m < 4; ++m)
        a[m] = *(const s16x8*)(As + (m * 16 + (lane & 15)) * A_STRIDE + (t * BK + ks * 32 + (lane >> 4) * 8) * 2);
#pragma unroll
      for (int n = 0; n < NTW; ++n) {
        const int tile = wid + 4 * n;
        if (tile < ntiles) {
          s16x8 b = tr_frag<BSTRIDE>(B, lane, ks * 32, tile * 16);
#pragma unroll
          for (int m = 0; m < 4; ++m) acc[m][n] = __builtin_amdgcn_mfma_f32_16x16x32_bf16(a[m], b, acc[m][n], 0, 0, 0);
        }
      }
    }
    if (t + 1 < NT) {
      b_store<NCH, NLD, BSTRIDE>(r, Bs + ((t + 1) & 1) * BUF);
      if (t + 2 < NT) b_load<NCH, NLD, MODE>(r, W, ldw, (t + 2) * BK, n0, nlim);
    }
    __syncthreads();
  }
}

__device__ __forceinline__ void ln_prologue(const u16* __restrict__ x, int m0, char* As) {
  const int row = threadIdx.x >> 2, part = threadIdx.x & 3;
  const u32x4* src = (const u32x4*)(x + (size_t)(m0 + row) * D_ + part * 160);
  u32x4 v[20];
#pragma unroll
  for (int i = 0; i < 20; ++i) v[i] = src[i];
  float s = 0.f;
#pragma unroll
  for (int i = 0; i < 20; ++i)
#pragma unroll
    for (int j = 0; j < 4; ++j) s += __uint_as_float(v[i][j] << 16) + __uint_as_float(v[i][j] & 0xFFFF0000u);
  s += __shfl_xor(s, 1); s += __shfl_xor(s, 2);
  const float mean = s * (1.f / D_);
  float q = 0.f;
#pragma unroll
  for (int i = 0; i < 20; ++i)
#pragma unroll
    for (int j = 0; j < 4; ++j) {
      float a = __uint_as_float(v[i][j] << 16) - mean, b = __uint_as_float(v[i][j] & 0xFFFF0000u) - mean;
      q += a * a + b * b;
    }
  q += __shfl_xor(q, 1); q += __shfl_xor(q, 2);
  const float rstd = 1.f / sqrtf(q * (1.f / D_) + 1e-5f);
  char* dst = As + row * A_STRIDE + part * 320;
#pragma unroll
  for (int i = 0; i < 20; ++i) {
    u32x4 o;
#pragma unroll
    for (int j = 0; j < 4; ++j)
      o[j] = pack2((__uint_as_float(v[i][j] << 16) - mean) * rstd, (__uint_as_float(v[i][j] & 0xFFFF0000u) - mean) * rstd);
    *(u32x4*)(dst + i * 16) = o;
  }
}

constexpr int K1_BSTRIDE = 544;
constexpr int K1_LDS = BM * A_STRIDE + 2 * BK * K1_BSTRIDE;
__global__ __launch_bounds__(NTHR, 1) void k_qkv(const u16* __restrict__ x, const u16* __restrict__ wqkv,
                                                 u16* __restrict__ pq, float* __restrict__ kvpart) {
  extern __shared__ __attribute__((aligned(16))) char smem[];
  char* As = smem;
  char* Bs = smem + BM * A_STRIDE;
  const int mt = blockIdx.x >> 3, hg = blockIdx.x & 7;
  const int m0 = mt * BM, c0 = hg * 80;
  const int lane = threadIdx.x & 63, wid = threadIdx.x >> 6;
  ln_prologue(x, m0, As);
  f32x4 acc[4][4] = {};
  mainloop_resA<30, 8, 1, K1_BSTRIDE, 4>(acc, As, Bs, wqkv, 3 * D_, c0, 3 * D_, 15);
  char* T = Bs;
#pragma unroll
  for (int n = 0; n < 4; ++n) {
    const int tile = wid + 4 * n;
    if (tile < 15) {
#pragma unroll
      for (int m = 0; m < 4; ++m)
#pragma unroll
        for (int j = 0; j < 4; ++j) {
          float v = acc[m][n][j];
          if (tile < 10) v = phi_f(v);
          const int r = m * 16 + (lane >> 4) * 4 + j;
          *(u16*)(T + perm_row(r) * K1_BSTRIDE + (tile * 16 + (lane & 15)) * 2) = f2bf(v);
        }
    }
  }
  __syncthreads();
  for (int id = threadIdx.x; id < BM * 10; id += NTHR) {
    const int r = id / 10, c = id % 10;
    *(u32x4*)(pq + (size_t)(m0 + r) * D_ + c0 + c * 8) = *(const u32x4*)(T + perm_row(r) * K1_BSTRIDE + c * 16);
  }
  f32x4 kv[2][2] = {};
#pragma unroll
  for (int ks = 0; ks < 2; ++ks) {
    s16x8 a0 = tr_frag<K1_BSTRIDE>(T, lane, ks * 32, 80 + 20 * wid);
    s16x8 a1 = tr_frag<K1_BSTRIDE>(T, lane, ks * 32, 96 + 20 * wid);
    s16x8 b0 = tr_frag<K1_BSTRIDE>(T, lane, ks * 32, 160 + 20 * wid);
    s16x8 b1 = tr_frag<K1_BSTRIDE>(T, lane, ks * 32, 176 + 20 * wid);
    if ((lane & 15) == 4) b1 = (s16x8){0x3F80, 0x3F80, 0x3F80, 0x3F80, 0x3F80, 0x3F80, 0x3F80, 0x3F80};
    kv[0][0] = __builtin_amdgcn_mfma_f32_16x16x32_bf16(a0, b0, kv[0][0], 0, 0, 0);
    kv[0][1] = __builtin_amdgcn_mfma_f32_16x16x32_bf16(a0, b1, kv[0][1], 0, 0, 0);
    kv[1][0] = __builtin_amdgcn_mfma_f32_16x16x32_bf16(a1, b0, kv[1][0], 0, 0, 0);
    kv[1][1] = __builtin_amdgcn_mfma_f32_16x16x32_bf16(a1, b1, kv[1][1], 0, 0, 0);
  }
  float* dst = kvpart + ((size_t)mt * H_ + hg * 4 + wid) * 20 * KV_LD;
#pragma unroll
  for (int dt = 0; dt < 2; ++dt)
#pragma unroll
    for (int et = 0; et < 2; ++et)
#pragma unroll
      for (int j = 0; j < 4; ++j) {
        const int d = dt * 16 + (lane >> 4) * 4 + j, e = et * 16 + (lane & 15);
        if (d < 20 && e <= 20) dst[d * KV_LD + e] = kv[dt][et][j];
      }
}

__global__ __launch_bounds__(NTHR, 1) void k_fold(const float* __restrict__ kvpart, const u16* __restrict__ wo,
                                                  u16* __restrict__ mcat, float* __restrict__ zout) {
  __shared__ float kvr[20 * KV_LD];
  const int h = blockIdx.x >> 3, n0 = (blockIdx.x & 7) * 80;
  for (int id = threadIdx.x; id < 20 * KV_LD; id += NTHR) {
    float s = 0.f;
    if ((id % KV_LD) <= 20) {
#pragma unroll 8
      for (int mt = 0; mt < S_ / BM; ++mt) s += kvpart[((size_t)mt * H_ + h) * 20 * KV_LD + id];
    }
    kvr[id] = s;
  }
  __syncthreads();
  if (n0 == 0 && threadIdx.x < 20) zout[h * 20 + threadIdx.x] = kvr[threadIdx.x * KV_LD + 20];
  for (int id = threadIdx.x; id < 20 * 80; id += NTHR) {
    const int d = id / 80, n = id % 80;
    float s = 0.f;
#pragma unroll
    for (int e = 0; e < 20; ++e) s += kvr[d * KV_LD + e] * bf2f(wo[(size_t)(h * 20 + e) * D_ + n0 + n]);
    mcat[(size_t)(h * 20 + d) * D_ + n0 + n] = f2bf(s);
  }
}

constexpr int K2_BSTRIDE = 288;
constexpr int K2_LDS = BM * A_STRIDE + 2 * BK * K2_BSTRIDE;
__global__ __launch_bounds__(NTHR, 1) void k_attn_out(const u16* __restrict__ pq, const float* __restrict__ z,
                                                      const u16* __restrict__ mcat, const u16* __restrict__ xin,
                                                      u16* __restrict__ xout) {
  extern __shared__ __attribute__((aligned(16))) char smem[];
  char* As = smem;
  char* Bs = smem + BM * A_STRIDE;
  const int mt = blockIdx.x >> 3, n0 = (blockIdx.x & 7) * 80, m0 = mt * BM;
  const int lane = threadIdx.x & 63, wid = threadIdx.x >> 6;
  {
    const int row = threadIdx.x >> 2, part = threadIdx.x & 3;
    const u16* src = pq + (size_t)(m0 + row) * D_ + part * 160;
    char* dst = As + row * A_STRIDE + part * 320;
#pragma unroll
    for (int hh = 0; hh < 8; ++hh) {
      const uint2* s2 = (const uint2*)(src + hh * 20);
      float p[20];
      float den = 1e-6f;
#pragma unroll
      for (int i = 0; i < 5; ++i) {
        uint2 w = s2[i];
        p[4 * i] = __uint_as_float(w.x << 16); p[4 * i + 1] = __uint_as_float(w.x & 0xFFFF0000u);
        p[4 * i + 2] = __uint_as_float(w.y << 16); p[4 * i + 3] = __uint_as_float(w.y & 0xFFFF0000u);
      }
#pragma unroll
      for (int d = 0; d < 20; ++d) den += p[d] * z[part * 160 + hh * 20 + d];
      const float inv = 1.f / den;
#pragma unroll
      for (int i = 0; i < 5; ++i) {
        uint2 o;
        o.x = pack2(p[4 * i] * inv, p[4 * i + 1] * inv);
        o.y = pack2(p[4 * i + 2] * inv, p[4 * i + 3] * inv);
        *(uint2*)(dst + hh * 40 + i * 8) = o;
      }
    }
  }
  f32x4 acc[4][2] = {};
  mainloop_resA<10, 3, 0, K2_BSTRIDE, 2>(acc, As, Bs, mcat, D_, n0, D_, 5);
#pragma unroll
  for (int n = 0; n < 2; ++n) {
    const int tile = wid + 4 * n;
    if (tile < 5) {
#pragma unroll
      for (int m = 0; m < 4; ++m)
#pragma unroll
        for (int j = 0; j < 4; ++j) {
          const size_t o = (size_t)(m0 + m * 16 + (lane >> 4) * 4 + j) * D_ + n0 + tile * 16 + (lane & 15);
          xout[o] = f2bf(bf2f(xin[o]) + acc[m][n][j]);
        }
    }
  }
}

constexpr int K3_BSTRIDE = 544;
constexpr int K3_LDS = BM * A_STRIDE + 2 * BK * K3_BSTRIDE;
__global__ __launch_bounds__(NTHR, 1) void k_ffn1(const u16* __restrict__ x, const u16* __restrict__ w1,
                                                  const u16* __restrict__ b1, u16* __restrict__ hbuf) {
  extern __shared__ __attribute__((aligned(16))) char smem[];
  char* As = smem;
  char* Bs = smem + BM * A_STRIDE;
  const int mt = blockIdx.x >> 3, n0 = (blockIdx.x & 7) * 224, m0 = mt * BM;
  const int lane = threadIdx.x & 63, wid = threadIdx.x >> 6;
  const int ntiles = min(14, (F_ - n0) / 16);
  ln_prologue(x, m0, As);
  f32x4 acc[4][4] = {};
  mainloop_resA<28, 7, 0, K3_BSTRIDE, 4>(acc, As, Bs, w1, F_, n0, F_, ntiles);
#pragma unroll
  for (int n = 0; n < 4; ++n) {
    const int tile = wid + 4 * n;
    if (tile < ntiles) {
      const int col = n0 + tile * 16 + (lane & 15);
      const float bb = bf2f(b1[col]);
#pragma unroll
      for (int m = 0; m < 4; ++m)
#pragma unroll
        for (int j = 0; j < 4; ++j)
          hbuf[(size_t)(m0 + m * 16 + (lane >> 4) * 4 + j) * F_ + col] = f2bf(gelu_f(acc[m][n][j] + bb));
    }
  }
}

constexpr int K4_BSTRIDE = 288;
constexpr int K4_ASTRIDE = BK * 2 + 16;
constexpr int K4_LDS = 2 * BM * K4_ASTRIDE + 2 * BK * K4_BSTRIDE;
__global__ __launch_bounds__(NTHR, 1) void k_ffn2(const u16* __restrict__ hbuf, const u16* __restrict__ w2,
                                                  const u16* __restrict__ b2, const u16* __restrict__ xin,
                                                  u16* __restrict__ xout) {
  extern __shared__ __attribute__((aligned(16))) char smem[];
  char* As = smem;
  char* Bs = smem + 2 * BM * K4_ASTRIDE;
  const int mt = blockIdx.x >> 3, n0 = (blockIdx.x & 7) * 80, m0 = mt * BM;
  const int lane = threadIdx.x & 63, wid = threadIdx.x >> 6;
  constexpr int NT = F_ / BK;
  f32x4 acc[4][2] = {};
  u32x4 rb[3], ra[2];
  auto a_load = [&](int k0) {
#pragma unroll
    for (int i = 0; i < 2; ++i) {
      const int id = threadIdx.x + i * NTHR;
      ra[i] = *(const u32x4*)(hbuf + (size_t)(m0 + (id >> 3)) * F_ + k0 + (id & 7) * 8);
    }
  };
  auto a_store = [&](char* dst) {
#pragma unroll
    for (int i = 0; i < 2; ++i) {
      const int id = threadIdx.x + i * NTHR;
      *(u32x4*)(dst + (id >> 3) * K4_ASTRIDE + (id & 7) * 16) = ra[i];
    }
  };
  b_load<10, 3, 0>(rb, w2, D_, 0, n0, D_);
  a_load(0);
  b_store<10, 3, K4_BSTRIDE>(rb, Bs);
  a_store(As);
  b_load<10, 3, 0>(rb, w2, D_, BK, n0, D_);
  a_load(BK);
  __syncthreads();
  for (int t = 0; t < NT; ++t) {
    const char* B = Bs + (t & 1) * BK * K4_BSTRIDE;
    const char* A = As + (t & 1) * BM * K4_ASTRIDE;
#pragma unroll
    for (int ks = 0; ks < 2; ++ks) {
      s16x8 a[4];
#pragma unroll
      for (int m = 0; m < 4; ++m)
        a[m] = *(const s16x8*)(A + (m * 16 + (lane & 15)) * K4_ASTRIDE + (ks * 32 + (lane >> 4) * 8) * 2);
#pragma unroll
      for (int n = 0; n < 2; ++n) {
        const int tile = wid + 4 * n;
        if (tile < 5) {
          s16x8 b = tr_frag<K4_BSTRIDE>(B, lane, ks * 32, tile * 16);
#pragma unroll
          for (int m = 0; m < 4; ++m) acc[m][n] = __builtin_amdgcn_mfma_f32_16x16x32_bf16(a[m], b, acc[m][n], 0, 0, 0);
        }
      }
    }
    if (t + 1 < NT) {
      b_store<10, 3, K4_BSTRIDE>(rb, Bs + ((t + 1) & 1) * BK * K4_BSTRIDE);
      a_store(As + ((t + 1) & 1) * BM * K4_ASTRIDE);
      if (t + 2 < NT) {
        b_load<10, 3, 0>(rb, w2, D_, (t + 2) * BK, n0, D_);
        a_load((t + 2) * BK);
      }
    }
    __syncthreads();
  }
#pragma unroll
  for (int n = 0; n < 2; ++n) {
    const int tile = wid + 4 * n;
    if (tile < 5) {
      const int col = n0 + tile * 16 + (lane & 15);
      const float bb = bf2f(b2[col]);
#pragma unroll
      for (int m = 0; m < 4; ++m)
#pragma unroll
        for (int j = 0; j < 4; ++j) {
          const size_t o = (size_t)(m0 + m * 16 + (lane >> 4) * 4 + j) * D_ + col;
          xout[o] = f2bf(bf2f(xin[o]) + acc[m][n][j] + bb);
        }
    }
  }
}

extern "C" void kernel_launch(void* const* d_in, const int* in_sizes, int n_in, void* d_out, int out_size, void* d_ws,
                              size_t ws_size, hipStream_t stream) {
  const u16* A_x = (const u16*)d_in[0];
  const u16* wqkv = (const u16*)d_in[1];
  const u16* wo = (const u16*)d_in[2];
  const u16* w1 = (const u16*)d_in[4];
  const u16* b1 = (const u16*)d_in[5];
  const u16* w2 = (const u16*)d_in[6];
  const u16* b2 = (const u16*)d_in[7];
  u16* out = (u16*)d_out;
  char* ws = (char*)d_ws;
  u16* xa = (u16*)ws;                              ws += (size_t)S_ * D_ * 2;
  u16* xb = (u16*)ws;                              ws += (size_t)S_ * D_ * 2;
  u16* pq = (u16*)ws;                              ws += (size_t)S_ * D_ * 2;
  u16* hbuf = (u16*)ws;                            ws += (size_t)S_ * F_ * 2;
  u16* mcat = (u16*)ws;                            ws += (size_t)D_ * D_ * 2;
  float* kvpart = (float*)ws;                      ws += (size_t)(S_ / BM) * H_ * 20 * KV_LD * 4;
  float* z = (float*)ws;

  hipFuncSetAttribute((const void*)k_qkv, hipFuncAttributeMaxDynamicSharedMemorySize, K1_LDS);
  hipFuncSetAttribute((const void*)k_attn_out, hipFuncAttributeMaxDynamicSharedMemorySize, K2_LDS);
  hipFuncSetAttribute((const void*)k_ffn1, hipFuncAttributeMaxDynamicSharedMemorySize, K3_LDS);
  hipFuncSetAttribute((const void*)k_ffn2, hipFuncAttributeMaxDynamicSharedMemorySize, K4_LDS);

  const u16* xcur = A_x;
  for (int l = 0; l < 2; ++l) {
    k_qkv<<<256, NTHR, K1_LDS, stream>>>(xcur, wqkv, pq, kvpart);
    k_fold<<<256, NTHR, 0, stream>>>(kvpart, wo, mcat, z);
    k_attn_out<<<256, NTHR, K2_LDS, stream>>>(pq, z, mcat, xcur, xa);
    k_ffn1<<<256, NTHR, K3_LDS, stream>>>(xa, w1, b1, hbuf);
    k_ffn2<<<256, NTHR, K4_LDS, stream>>>(hbuf, w2, b2, xa, l == 1 ? out : xb);
    xcur = xb;
  }
}
```

```cpp
#include <hip/hip_runtime.h>
#include <hip/hip_bf16.h>
#include <stdint.h>

typedef unsigned short u16;
typedef __attribute__((ext_vector_type(4))) short s16x4;
typedef __attribute__((ext_vector_type(8))) short s16x8;
typedef __attribute__((ext_vector_type(4))) float f32x4;
typedef __attribute__((ext_vector_type(4))) unsigned u32x4;
typedef __attribute__((address_space(3))) s16x4* lds_s16x4_p;

constexpr int S_ = 2048, D_ = 640, H_ = 32, F_ = 1728;
constexpr int BM = 64, BK = 64, NTHR = 256;
constexpr int A_STRIDE = D_ * 2 + 16;
constexpr int KV_LD = 24;

__device__ __forceinline__ float bf2f(u16 v) { return __uint_as_float(((unsigned)v) << 16); }
__device__ __forceinline__ u16 f2bf(float f) {
  unsigned u = __float_as_uint(f);
  return (u16)((u + 0x7FFFu + ((u >> 16) & 1u)) >> 16);
}
__device__ __forceinline__ unsigned pack2(float a, float b) { return (unsigned)f2bf(a) | ((unsigned)f2bf(b) << 16); }
__device__ __forceinline__ float phi_f(float t) { return (t > 0.f ? t + 1.f : __expf(t)) + 1e-6f; }
__device__ __forceinline__ float gelu_f(float x) {
  float u = 0.7978845608028654f * (x + 0.044715f * x * x * x);
  float e = __expf(2.f * u);
  float th = 1.f - 2.f / (e + 1.f);
  return 0.5f * x * (1.f + th);
}
__device__ __forceinline__ int perm_row(int r) { return (r & ~12) | ((r & 4) << 1) | ((r & 8) >> 1); }

template <int BSTRIDE>
__device__ __forceinline__ s16x8 tr_frag(const char* img, int lane, int krow0, int col0) {
  const int g = lane >> 4, q = (lane & 15) >> 2, p = lane & 3;
  const int r = krow0 + q + 4 * (g & 1) + 16 * (g >> 1);
  const char* a0 = img + r * BSTRIDE + (col0 + 4 * p) * 2;
  s16x4 lo = __builtin_amdgcn_ds_read_tr16_b64_v4i16((lds_s16x4_p)(a0));
  s16x4 hi = __builtin_amdgcn_ds_read_tr16_b64_v4i16((lds_s16x4_p)(a0 + 8 * BSTRIDE));
  return __builtin_shufflevector(lo, hi, 0, 1, 2, 3, 4, 5, 6, 7);
}

template <int NCH, int NLD, int MODE>
__device__ __forceinline__ void b_load(u32x4 (&r)[NLD], const u16* __restrict__ W, int ldw, int k0, int n0, int nlim) {
#pragma unroll
  for (int i = 0; i < NLD; ++i) {
    const int id = threadIdx.x + i * NTHR;
    const int row = id / NCH, c = id % NCH;
    int gcol;
    if (MODE == 1) gcol = (c / 10) * D_ + n0 + (c % 10) * 8;
    else gcol = n0 + c * 8;
    u32x4 v = {0u, 0u, 0u, 0u};
    v = *(const u32x4*)(W + (size_t)(k0 + min(row, BK - 1)) * ldw + min(gcol, nlim - 8));
    r[i] = v;
  }
}
template <int NCH, int NLD, int BSTRIDE>
__device__ __forceinline__ void b_store(const u32x4 (&r)[NLD], char* img) {
#pragma unroll
  for (int i = 0; i < NLD; ++i) {
    const int id = threadIdx.x + i * NTHR;
    const int row = id / NCH, c = id % NCH;
    if (id < BK * NCH) *(u32x4*)(img + perm_row(row) * BSTRIDE + c * 16) = r[i];
  }
}

template <int NCH, int NLD, int MODE, int BSTRIDE, int NTW>
__device__ __forceinline__ void mainloop_resA(f32x4 (&acc)[4][NTW], const char* As, char* Bs, const u16* __restrict__ W,
                                              int ldw, int n0, int nlim, int ntiles) {
  const int lane = threadIdx.x & 63, wid = threadIdx.x >> 6;
  constexpr int NT = D_ / BK;
  constexpr int BUF = BK * BSTRIDE;
  u32x4 r[NLD];
  b_load<NCH, NLD, MODE>(r, W, ldw, 0, n0, nlim);
  b_store<NCH, NLD, BSTRIDE>(r, Bs);
  b_load<NCH, NLD, MODE>(r, W, ldw, BK, n0, nlim);
  __syncthreads();
  for (int t = 0; t < NT; ++t) {
    const char* B = Bs + (t & 1) * BUF;
#pragma unroll
    for (int ks = 0; ks < 2; ++ks) {
      s16x8 a[4];
#pragma unroll
      for (int m = 0; m < 4; ++m)
        a[m] = *(const s16x8*)(As + (m * 16 + (lane & 15)) * A_STRIDE + (t * BK + ks * 32 + (lane >> 4) * 8) * 2);
#pragma unroll
      for (int n = 0; n < NTW; ++n) {
        const int tile = min(wid + 4 * n, ntiles - 1);
        {
          s16x8 b = tr_frag<BSTRIDE>(B, lane, ks * 32, tile * 16);
#pragma unroll
          for (int m = 0; m < 4; ++m) acc[m][n] = __builtin_amdgcn_mfma_f32_16x16x32_bf16(a[m], b, acc[m][n], 0, 0, 0);
        }
      }
    }
    if (t + 1 < NT) {
      b_store<NCH, NLD, BSTRIDE>(r, Bs + ((t + 1) & 1) * BUF);
      if (t + 2 < NT) b_load<NCH, NLD, MODE>(r, W, ldw, (t + 2) * BK, n0, nlim);
    }
    __syncthreads();
  }
}

__device__ __forceinline__ void ln_prologue(const u16* __restrict__ x, int m0, char* As) {
  const int row = threadIdx.x >> 2, part = threadIdx.x & 3;
  const u32x4* src = (const u32x4*)(x + (size_t)(m0 + row) * D_ + part * 160);
  u32x4 v[20];
#pragma unroll
  for (int i = 0; i < 20; ++i) v[i] = src[i];
  float s = 0.f;
#pragma unroll
  for (int i = 0; i < 20; ++i)
#pragma unroll
    for (int j = 0; j < 4; ++j) s += __uint_as_float(v[i][j] << 16) + __uint_as_float(v[i][j] & 0xFFFF0000u);
  s += __shfl_xor(s, 1); s += __shfl_xor(s, 2);
  const float mean = s * (1.f / D_);
  float q = 0.f;
#pragma unroll
  for (int i = 0; i < 20; ++i)
#pragma unroll
    for (int j = 0; j < 4; ++j) {
      float a = __uint_as_float(v[i][j] << 16) - mean, b = __uint_as_float(v[i][j] & 0xFFFF0000u) - mean;
      q += a * a + b * b;
    }
  q += __shfl_xor(q, 1); q += __shfl_xor(q, 2);
  const float rstd = 1.f / sqrtf(q * (1.f / D_) + 1e-5f);
  char* dst = As + row * A_STRIDE + part * 320;
#pragma unroll
  for (int i = 0; i < 20; ++i) {
    u32x4 o;
#pragma unroll
    for (int j = 0; j < 4; ++j)
      o[j] = pack2((__uint_as_float(v[i][j] << 16) - mean) * rstd, (__uint_as_float(v[i][j] & 0xFFFF0000u) - mean) * rstd);
    *(u32x4*)(dst + i * 16) = o;
  }
}

constexpr int K1_BSTRIDE = 544;
constexpr int K1_LDS = BM * A_STRIDE + 2 * BK * K1_BSTRIDE;
__global__ __launch_bounds__(NTHR, 1) void k_qkv(const u16* __restrict__ x, const u16* __restrict__ wqkv,
                                                 u16* __restrict__ pq, float* __restrict__ kvpart) {
  extern __shared__ __attribute__((aligned(16))) char smem[];
  char* As = smem;
  char* Bs = smem + BM * A_STRIDE;
  const int mt = blockIdx.x >> 3, hg = blockIdx.x & 7;
  const int m0 = mt * BM, c0 = hg * 80;
  const int lane = threadIdx.x & 63, wid = threadIdx.x >> 6;
  ln_prologue(x, m0, As);
  f32x4 acc[4][4] = {};
  mainloop_resA<30, 8, 1, K1_BSTRIDE, 4>(acc, As, Bs, wqkv, 3 * D_, c0, 3 * D_, 15);
  char* T = Bs;
#pragma unroll
  for (int n = 0; n < 4; ++n) {
    const int tile = wid + 4 * n;
    if (tile < 15) {
#pragma unroll
      for (int m = 0; m < 4; ++m)
#pragma unroll
        for (int j = 0; j < 4; ++j) {
          float v = acc[m][n][j];
          if (tile < 10) v = phi_f(v);
          const int r = m * 16 + (lane >> 4) * 4 + j;
          *(u16*)(T + perm_row(r) * K1_BSTRIDE + (tile * 16 + (lane & 15)) * 2) = f2bf(v);
        }
    }
  }
  __syncthreads();
  for (int id = threadIdx.x; id < BM * 10; id += NTHR) {
    const int r = id / 10, c = id % 10;
    *(u32x4*)(pq + (size_t)(m0 + r) * D_ + c0 + c * 8) = *(const u32x4*)(T + perm_row(r) * K1_BSTRIDE + c * 16);
  }
  f32x4 kv[2][2] = {};
#pragma unroll
  for (int ks = 0; ks < 2; ++ks) {
    s16x8 a0 = tr_frag<K1_BSTRIDE>(T, lane, ks * 32, 80 + 20 * wid);
    s16x8 a1 = tr_frag<K1_BSTRIDE>(T, lane, ks * 32, 96 + 20 * wid);
    s16x8 b0 = tr_frag<K1_BSTRIDE>(T, lane, ks * 32, 160 + 20 * wid);
    s16x8 b1 = tr_frag<K1_BSTRIDE>(T, lane, ks * 32, 176 + 20 * wid);
    if ((lane & 15) == 4) b1 = (s16x8){0x3F80, 0x3F80, 0x3F80, 0x3F80, 0x3F80, 0x3F80, 0x3F80, 0x3F80};
    kv[0][0] = __builtin_amdgcn_mfma_f32_16x16x32_bf16(a0, b0, kv[0][0], 0, 0, 0);
    kv[0][1] = __builtin_amdgcn_mfma_f32_16x16x32_bf16(a0, b1, kv[0][1], 0, 0, 0);
    kv[1][0] = __builtin_amdgcn_mfma_f32_16x16x32_bf16(a1, b0, kv[1][0], 0, 0, 0);
    kv[1][1] = __builtin_amdgcn_mfma_f32_16x16x32_bf16(a1, b1, kv[1][1], 0, 0, 0);
  }
  float* dst = kvpart + ((size_t)mt * H_ + hg * 4 + wid) * 20 * KV_LD;
#pragma unroll
  for (int dt = 0; dt < 2; ++dt)
#pragma unroll
    for (int et = 0; et < 2; ++et)
#pragma unroll
      for (int j = 0; j < 4; ++j) {
        const int d = dt * 16 + (lane >> 4) * 4 + j, e = et * 16 + (lane & 15);
        if (d < 20 && e <= 20) dst[d * KV_LD + e] = kv[dt][et][j];
      }
}

__global__ __launch_bounds__(NTHR, 1) void k_fold(const float* __restrict__ kvpart, const u16* __restrict__ wo,
                                                  u16* __restrict__ mcat, float* __restrict__ zout) {
  __shared__ float kvr[20 * KV_LD];
  const int h = blockIdx.x >> 3, n0 = (blockIdx.x & 7) * 80;
  for (int id = threadIdx.x; id < 20 * KV_LD; id += NTHR) {
    float s = 0.f;
    if ((id % KV_LD) <= 20) {
#pragma unroll 8
      for (int mt = 0; mt < S_ / BM; ++mt) s += kvpart[((size_t)mt * H_ + h) * 20 * KV_LD + id];
    }
    kvr[id] = s;
  }
  __syncthreads();
  if (n0 == 0 && threadIdx.x < 20) zout[h * 20 + threadIdx.x] = kvr[threadIdx.x * KV_LD + 20];
  for (int id = threadIdx.x; id < 20 * 80; id += NTHR) {
    const int d = id / 80, n = id % 80;
    float s = 0.f;
#pragma unroll
    for (int e = 0; e < 20; ++e) s += kvr[d * KV_LD + e] * bf2f(wo[(size_t)(h * 20 + e) * D_ + n0 + n]);
    mcat[(size_t)(h * 20 + d) * D_ + n0 + n] = f2bf(s);
  }
}

constexpr int K2_BSTRIDE = 288;
constexpr int K2_LDS = BM * A_STRIDE + 2 * BK * K2_BSTRIDE;
__global__ __launch_bounds__(NTHR, 1) void k_attn_out(const u16* __restrict__ pq, const float* __restrict__ z,
                                                      const u16* __restrict__ mcat, const u16* __restrict__ xin,
                                                      u16* __restrict__ xout) {
  extern __shared__ __attribute__((aligned(16))) char smem[];
  char* As = smem;
  char* Bs = smem + BM * A_STRIDE;
  const int mt = blockIdx.x >> 3, n0 = (blockIdx.x & 7) * 80, m0 = mt * BM;
  const int lane = threadIdx.x & 63, wid = threadIdx.x >> 6;
  {
    const int row = threadIdx.x >> 2, part = threadIdx.x & 3;
    const u16* src = pq + (size_t)(m0 + row) * D_ + part * 160;
    char* dst = As + row * A_STRIDE + part * 320;
#pragma unroll
    for (int hh = 0; hh < 8; ++hh) {
      const uint2* s2 = (const uint2*)(src + hh * 20);
      float p[20];
      float den = 1e-6f;
#pragma unroll
      for (int i = 0; i < 5; ++i) {
        uint2 w = s2[i];
        p[4 * i] = __uint_as_float(w.x << 16); p[4 * i + 1] = __uint_as_float(w.x & 0xFFFF0000u);
        p[4 * i + 2] = __uint_as_float(w.y << 16); p[4 * i + 3] = __uint_as_float(w.y & 0xFFFF0000u);
      }
#pragma unroll
      for (int d = 0; d < 20; ++d) den += p[d] * z[part * 160 + hh * 20 + d];
      const float inv = 1.f / den;
#pragma unroll
      for (int i = 0; i < 5; ++i) {
        uint2 o;
        o.x = pack2(p[4 * i] * inv, p[4 * i + 1] * inv);
        o.y = pack2(p[4 * i + 2] * inv, p[4 * i + 3] * inv);
        *(uint2*)(dst + hh * 40 + i * 8) = o;
      }
    }
  }
  f32x4 acc[4][2] = {};
  mainloop_resA<10, 3, 0, K2_BSTRIDE, 2>(acc, As, Bs, mcat, D_, n0, D_, 5);
#pragma unroll
  for (int n = 0; n < 2; ++n) {
    const int tile = wid + 4 * n;
    if (tile < 5) {
#pragma unroll
      for (int m = 0; m < 4; ++m)
#pragma unroll
        for (int j = 0; j < 4; ++j) {
          const size_t o = (size_t)(m0 + m * 16 + (lane >> 4) * 4 + j) * D_ + n0 + tile * 16 + (lane & 15);
          xout[o] = f2bf(bf2f(xin[o]) + acc[m][n][j]);
        }
    }
  }
}

constexpr int K3_BSTRIDE = 544;
constexpr int K3_LDS = BM * A_STRIDE + 2 * BK * K3_BSTRIDE;
__global__ __launch_bounds__(NTHR, 1) void k_ffn1(const u16* __restrict__ x, const u16* __restrict__ w1,
                                                  const u16* __restrict__ b1, u16* __restrict__ hbuf) {
  extern __shared__ __attribute__((aligned(16))) char smem[];
  char* As = smem;
  char* Bs = smem + BM * A_STRIDE;
  const int mt = blockIdx.x >> 3, n0 = (blockIdx.x & 7) * 224, m0 = mt * BM;
  const int lane = threadIdx.x & 63, wid = threadIdx.x >> 6;
  const int ntiles = min(14, (F_ - n0) / 16);
  ln_prologue(x, m0, As);
  f32x4 acc[4][4] = {};
  mainloop_resA<28, 7, 0, K3_BSTRIDE, 4>(acc, As, Bs, w1, F_, n0, F_, ntiles);
#pragma unroll
  for (int n = 0; n < 4; ++n) {
    const int tile = wid + 4 * n;
    if (tile < ntiles) {
      const int col = n0 + tile * 16 + (lane & 15);
      const float bb = bf2f(b1[col]);
#pragma unroll
      for (int m = 0; m < 4; ++m)
#pragma unroll
        for (int j = 0; j < 4; ++j)
          hbuf[(size_t)(m0 + m * 16 + (lane >> 4) * 4 + j) * F_ + col] = f2bf(gelu_f(acc[m][n][j] + bb));
    }
  }
}

constexpr int K4_BSTRIDE = 288;
constexpr int K4_ASTRIDE = BK * 2 + 16;
constexpr int K4_LDS = 2 * BM * K4_ASTRIDE + 2 * BK * K4_BSTRIDE;
__global__ __launch_bounds__(NTHR, 1) void k_ffn2(const u16* __restrict__ hbuf, const u16* __restrict__ w2,
                                                  const u16* __restrict__ b2, const u16* __restrict__ xin,
                                                  u16* __restrict__ xout) {
  extern __shared__ __attribute__((aligned(16))) char smem[];
  char* As = smem;
  char* Bs = smem + 2 * BM * K4_ASTRIDE;
  const int mt = blockIdx.x >> 3, n0 = (blockIdx.x & 7) * 80, m0 = mt * BM;
  const int lane = threadIdx.x & 63, wid = threadIdx.x >> 6;
  constexpr int NT = F_ / BK;
  f32x4 acc[4][2] = {};
  u32x4 rb[3], ra[2];
  auto a_load = [&](int k0) {
#pragma unroll
    for (int i = 0; i < 2; ++i) {
      const int id = threadIdx.x + i * NTHR;
      ra[i] = *(const u32x4*)(hbuf + (size_t)(m0 + (id >> 3)) * F_ + k0 + (id & 7) * 8);
    }
  };
  auto a_store = [&](char* dst) {
#pragma unroll
    for (int i = 0; i < 2; ++i) {
      const int id = threadIdx.x + i * NTHR;
      *(u32x4*)(dst + (id >> 3) * K4_ASTRIDE + (id & 7) * 16) = ra[i];
    }
  };
  b_load<10, 3, 0>(rb, w2, D_, 0, n0, D_);
  a_load(0);
  b_store<10, 3, K4_BSTRIDE>(rb, Bs);
  a_store(As);
  b_load<10, 3, 0>(rb, w2, D_, BK, n0, D_);
  a_load(BK);
  __syncthreads();
  for (int t = 0; t < NT; ++t) {
    const char* B = Bs + (t & 1) * BK * K4_BSTRIDE;
    const char* A = As + (t & 1) * BM * K4_ASTRIDE;
#pragma unroll
    for (int ks = 0; ks < 2; ++ks) {
      s16x8 a[4];
#pragma unroll
      for (int m = 0; m < 4; ++m)
        a[m] = *(const s16x8*)(A + (m * 16 + (lane & 15)) * K4_ASTRIDE + (ks * 32 + (lane >> 4) * 8) * 2);
#pragma unroll
      for (int n = 0; n < 2; ++n) {
        const int tile = min(wid + 4 * n, 4);
        {
          s16x8 b = tr_frag<K4_BSTRIDE>(B, lane, ks * 32, tile * 16);
#pragma unroll
          for (int m = 0; m < 4; ++m) acc[m][n] = __builtin_amdgcn_mfma_f32_16x16x32_bf16(a[m], b, acc[m][n], 0, 0, 0);
        }
      }
    }
    if (t + 1 < NT) {
      b_store<10, 3, K4_BSTRIDE>(rb, Bs + ((t + 1) & 1) * BK * K4_BSTRIDE);
      a_store(As + ((t + 1) & 1) * BM * K4_ASTRIDE);
      if (t + 2 < NT) {
        b_load<10, 3, 0>(rb, w2, D_, (t + 2) * BK, n0, D_);
        a_load((t + 2) * BK);
      }
    }
    __syncthreads();
  }
#pragma unroll
  for (int n = 0; n < 2; ++n) {
    const int tile = wid + 4 * n;
    if (tile < 5) {
      const int col = n0 + tile * 16 + (lane & 15);
      const float bb = bf2f(b2[col]);
#pragma unroll
      for (int m = 0; m < 4; ++m)
#pragma unroll
        for (int j = 0; j < 4; ++j) {
          const size_t o = (size_t)(m0 + m * 16 + (lane >> 4) * 4 + j) * D_ + col;
          xout[o] = f2bf(bf2f(xin[o]) + acc[m][n][j] + bb);
        }
    }
  }
}

extern "C" void kernel_launch(void* const* d_in, const int* in_sizes, int n_in, void* d_out, int out_size, void* d_ws,
                              size_t ws_size, hipStream_t stream) {
  const u16* A_x = (const u16*)d_in[0];
  const u16* wqkv = (const u16*)d_in[1];
  const u16* wo = (const u16*)d_in[2];
  const u16* w1 = (const u16*)d_in[4];
  const u16* b1 = (const u16*)d_in[5];
  const u16* w2 = (const u16*)d_in[6];
  const u16* b2 = (const u16*)d_in[7];
  u16* out = (u16*)d_out;
  char* ws = (char*)d_ws;
  u16* xa = (u16*)ws;                              ws += (size_t)S_ * D_ * 2;
  u16* xb = (u16*)ws;                              ws += (size_t)S_ * D_ * 2;
  u16* pq = (u16*)ws;                              ws += (size_t)S_ * D_ * 2;
  u16* hbuf = (u16*)ws;                            ws += (size_t)S_ * F_ * 2;
  u16* mcat = (u16*)ws;                            ws += (size_t)D_ * D_ * 2;
  float* kvpart = (float*)ws;                      ws += (size_t)(S_ / BM) * H_ * 20 * KV_LD * 4;
  float* z = (float*)ws;

  hipFuncSetAttribute((const void*)k_qkv, hipFuncAttributeMaxDynamicSharedMemorySize, K1_LDS);
  hipFuncSetAttribute((const void*)k_attn_out, hipFuncAttributeMaxDynamicSharedMemorySize, K2_LDS);
  hipFuncSetAttribute((const void*)k_ffn1, hipFuncAttributeMaxDynamicSharedMemorySize, K3_LDS);
  hipFuncSetAttribute((const void*)k_ffn2, hipFuncAttributeMaxDynamicSharedMemorySize, K4_LDS);

  const u16* xcur = A_x;
  for (int l = 0; l < 2; ++l) {
    k_qkv<<<256, NTHR, K1_LDS, stream>>>(xcur, wqkv, pq, kvpart);
    k_fold<<<256, NTHR, 0, stream>>>(kvpart, wo, mcat, z);
    k_attn_out<<<256, NTHR, K2_LDS, stream>>>(pq, z, mcat, xcur, xa);
    k_ffn1<<<256, NTHR, K3_LDS, stream>>>(xa, w1, b1, hbuf);
    k_ffn2<<<256, NTHR, K4_LDS, stream>>>(hbuf, w2, b2, xa, l == 1 ? out : xb);
    xcur = xb;
  }
}
```
